```python
import torch

def _softmax(x, axis=-1):
    return torch.softmax(x, dim=axis)
def _gelu(x):
    return torch.nn.functional.gelu(x, approximate="tanh")
def _silu(x):
    return torch.nn.functional.silu(x)
def _sigmoid(x):
    return torch.sigmoid(x)
def _layernorm(x, axis=-1, eps=1e-5):
    m = x.mean(dim=axis, keepdim=True)
    v = x.var(dim=axis, keepdim=True, unbiased=False)
    return (x - m) / torch.sqrt(v + eps)
def _scatter_add(tgt, idx, src, axis=0):
    return tgt.clone().index_add_(axis, idx, src)
def _segment_sum(src, seg, n):
    out = torch.zeros((n,) + src.shape[1:], dtype=src.dtype)
    return out.index_add_(0, seg, src)
def _pad(x, pw):
    flat = tuple(v for pair in reversed(pw) for v in pair)
    return torch.nn.functional.pad(x, flat)

kernel_name = 'standalone__kv_cache_quant_int4_int2_kv_with_per_channel_or_per_token_scales__sampling'

def setup_inputs():

    _w = (lambda a: (lambda t: t.to(torch.bfloat16) if t.is_floating_point() else t)(torch.as_tensor(a)))
    B = 16
    Sq = 16
    Skv = 4096
    Hq = 32
    Hkv = 4
    Dh = 128
    bits = 2
    G = 32
    if G <= 0 or G >= Skv:
        G = max(1, Skv // 2)
    Skv = (Skv // G) * G
    ngk = Skv // G
    qmax = (1 << bits) - 1
    return {
        "q": _w((lambda *s: torch.randn(*s, dtype=torch.bfloat16) * 3.2)(B, Hq, Sq, Dh)),
        "qk": _w(torch.randint(0, qmax + 1, (B, Hkv, Skv, Dh)).to(torch.int32)),
        "qv": _w(torch.randint(0, qmax + 1, (B, Hkv, Skv, Dh)).to(torch.int32)),
        "k_scales": _w(torch.abs((lambda *s: torch.randn(*s, dtype=torch.bfloat16) * 3.2)(B, Hkv, ngk, Dh)) * 0.05 + 0.02),
        "k_zeros": _w((lambda *s: torch.randn(*s, dtype=torch.bfloat16) * 3.2)(B, Hkv, ngk, Dh) + float(qmax) / 2.0),
        "v_scales": _w(torch.abs((lambda *s: torch.randn(*s, dtype=torch.bfloat16) * 0.05)(B, Hkv, Skv, 1)) * 0.05 + 0.02),
        "v_zeros": _w((lambda *s: torch.randn(*s, dtype=torch.bfloat16) * 0.05)(B, Hkv, Skv, 1) + float(qmax) / 2.0),
    }

def reference(q, qk, qv, k_scales, k_zeros, v_scales, v_zeros):
    B, Hq, Sq, Dh = q.shape
    Hkv, Skv = qk.shape[1], qk.shape[2]
    ngk = k_scales.shape[2]
    G = Skv // ngk
    ks = (lambda x, n, axis: torch.repeat_interleave(x, n, dim=axis))(k_scales, G, axis=2)
    kz = (lambda x, n, axis: torch.repeat_interleave(x, n, dim=axis))(k_zeros, G, axis=2)
    k = ((lambda x, dt: x.to(dt))(qk, q.dtype) - kz) * ks
    v = ((lambda x, dt: x.to(dt))(qv, q.dtype) - v_zeros) * v_scales
    rep = Hq // Hkv
    if rep > 1:
        k = (lambda x, n, axis: torch.repeat_interleave(x, n, dim=axis))(k, rep, axis=1)
        v = (lambda x, n, axis: torch.repeat_interleave(x, n, dim=axis))(v, rep, axis=1)
    scale = float(Dh) ** -0.5
    logits = torch.einsum("bhqd,bhkd->bhqk", q, k) * scale
    p = _softmax(logits, axis=-1)
    o = torch.einsum("bhqk,bhkd->bhqd", p, v)
    return o
```

```cpp
#include <hip/hip_runtime.h>
#include <hip/hip_bf16.h>

namespace {

constexpr int NB = 16, HQ = 32, HKV = 4, SQ = 16, SKV = 4096, DH = 128, NGK = 128;
constexpr int NSPLIT = 4;
constexpr int KEYS = SKV / NSPLIT;
constexpr int TILE = 64;
constexpr int NT = KEYS / TILE;
constexpr int RING = 4;
constexpr int NWG = NB * HKV * NSPLIT;
constexpr float SCALE_L2E = 0.08838834764831845f * 1.4426950408889634f;

#define KV_AUX 2

using bf16x8 = __attribute__((ext_vector_type(8))) short;
using f32x4 = __attribute__((ext_vector_type(4))) float;
using u32x4 = __attribute__((ext_vector_type(4))) unsigned;
using u32x2 = __attribute__((ext_vector_type(2))) unsigned;
using bf16x2 = __attribute__((ext_vector_type(2))) __bf16;
using f32x2 = __attribute__((ext_vector_type(2))) float;

constexpr int L_K = 0;
constexpr int L_V = 32768;
constexpr int L_KS = 65536;
constexpr int L_KZ = L_KS + 16384;
constexpr int L_VS = L_KZ + 16384;
constexpr int L_VZ = L_VS + 4096;
constexpr int L_END = L_VZ + 4096;

constexpr size_t WS_PO = 0;
constexpr size_t WS_ML = WS_PO + (size_t)NWG * 8 * 8 * 64 * 16;

__device__ __forceinline__ unsigned pk2(float lo, float hi) {
  f32x2 v = {lo, hi};
  bf16x2 r = __builtin_convertvector(v, bf16x2);
  return __builtin_bit_cast(unsigned, r);
}
__device__ __forceinline__ float bf_lo(unsigned w) { return __builtin_bit_cast(float, w << 16); }
__device__ __forceinline__ float bf_hi(unsigned w) { return __builtin_bit_cast(float, w & 0xffff0000u); }

__device__ __forceinline__ float xrow_max(float x) {
  auto s = __builtin_amdgcn_permlane16_swap(__float_as_uint(x), __float_as_uint(x), false, false);
  x = fmaxf(__uint_as_float(s[0]), __uint_as_float(s[1]));
  auto t = __builtin_amdgcn_permlane32_swap(__float_as_uint(x), __float_as_uint(x), false, false);
  return fmaxf(__uint_as_float(t[0]), __uint_as_float(t[1]));
}
__device__ __forceinline__ float xrow_sum(float x) {
  auto s = __builtin_amdgcn_permlane16_swap(__float_as_uint(x), __float_as_uint(x), false, false);
  x = __uint_as_float(s[0]) + __uint_as_float(s[1]);
  auto t = __builtin_amdgcn_permlane32_swap(__float_as_uint(x), __float_as_uint(x), false, false);
  return __uint_as_float(t[0]) + __uint_as_float(t[1]);
}

template <bool IS_K>
__device__ __forceinline__ void body(char* __restrict__ lds, __amdgpu_buffer_rsrc_t rs, const unsigned short* __restrict__ q,
                                     const unsigned short* __restrict__ ksc, const unsigned short* __restrict__ kzr,
                                     const unsigned short* __restrict__ vsc, const unsigned short* __restrict__ vzr,
                                     char* __restrict__ ws, int wg, int bh, int split, int wave, int tid) {
  const int lane = tid & 63;
  const int sub = wave & 3;
  const size_t kt_base = ((size_t)bh * NGK + (size_t)split * (KEYS / 32)) * DH;
  const u32x4 tks = *reinterpret_cast<const u32x4*>(ksc + kt_base + (size_t)tid * 8);
  const u32x4 tkz = *reinterpret_cast<const u32x4*>(kzr + kt_base + (size_t)tid * 8);
  const size_t vt_base = (size_t)bh * SKV + (size_t)split * KEYS;
  const unsigned tvs = *reinterpret_cast<const unsigned*>(vsc + vt_base + (size_t)tid * 2);
  const unsigned tvz = *reinterpret_cast<const unsigned*>(vzr + vt_base + (size_t)tid * 2);
  const int qi = lane & 15, g = lane >> 4;
  bf16x8 qf[4];
  {
    const unsigned short* qrow = q + ((size_t)(bh * 8 + wave) * SQ + qi) * DH;
#pragma unroll
    for (int ks = 0; ks < 4; ++ks) {
      const u32x2 a = *reinterpret_cast<const u32x2*>(qrow + 4 * (4 * ks + g));
      const u32x2 b = *reinterpret_cast<const u32x2*>(qrow + 64 + 4 * (4 * ks + g));
      u32x4 w = {a[0], a[1], b[0], b[1]};
      qf[ks] = __builtin_bit_cast(bf16x8, w);
    }
  }
  int voff0, voff1;
  if (IS_K) {
    const int c = lane & 15, rr = lane >> 4;
    voff0 = ((16 * sub + rr) * DH + 4 * c) * 4;
    voff1 = voff0 + 4096;
  } else {
    const int c = lane & 31, h = lane >> 5;
    voff0 = ((32 * (sub >> 1) + 4 * (2 * (sub & 1) + h)) * DH + 4 * c) * 4;
    voff1 = voff0 + 8192;
  }
  u32x4 raw[RING][8];
  auto issue = [&](u32x4 (&r)[8], int t) {
    const int so = t * (TILE * DH * 4);
#pragma unroll
    for (int j = 0; j < 8; ++j) {
      const int imm = IS_K ? (((j >> 1) & 1) * 2048 + (j & 1) * 256) : ((j & 3) * 512);
      const int vo = (j < 4) ? voff0 : voff1;
      r[j] = __builtin_amdgcn_raw_buffer_load_b128(rs, vo + imm, so, KV_AUX);
    }
  };
#pragma unroll
  for (int u = 0; u < RING; ++u) issue(raw[u], u);
  __builtin_amdgcn_sched_barrier(0);

  {
    float s[8], z[8];
#pragma unroll
    for (int e = 0; e < 4; ++e) {
      s[2 * e] = bf_lo(tks[e]); s[2 * e + 1] = bf_hi(tks[e]);
      z[2 * e] = -bf_lo(tkz[e]) * s[2 * e]; z[2 * e + 1] = -bf_hi(tkz[e]) * s[2 * e + 1];
    }
    f32x4* ps = reinterpret_cast<f32x4*>(lds + L_KS + tid * 32);
    f32x4* pz = reinterpret_cast<f32x4*>(lds + L_KZ + tid * 32);
    ps[0] = f32x4{s[0], s[1], s[2], s[3]}; ps[1] = f32x4{s[4], s[5], s[6], s[7]};
    pz[0] = f32x4{z[0], z[1], z[2], z[3]}; pz[1] = f32x4{z[4], z[5], z[6], z[7]};
    const float s0 = bf_lo(tvs), s1 = bf_hi(tvs);
    *reinterpret_cast<f32x2*>(lds + L_VS + tid * 8) = f32x2{s0, s1};
    *reinterpret_cast<f32x2*>(lds + L_VZ + tid * 8) = f32x2{-bf_lo(tvz) * s0, -bf_hi(tvz) * s1};
  }
  __syncthreads();

  int wr_off, tab_off;
  if (IS_K) {
    const int c = lane & 15, rr = lane >> 4;
    wr_off = L_K + ((sub * 4 + (c >> 2)) * 64 + 16 * (c & 3) + (rr ^ (c & 7))) * 16;
    tab_off = (sub >> 1) * 512 + c * 16;
  } else {
    const int c = lane & 31, h = lane >> 5;
    wr_off = L_V + (((sub >> 1) * 8 + 4 * (c >> 4)) * 64 + (c & 15) + 16 * (2 * (sub & 1) + h)) * 16;
    tab_off = (32 * (sub >> 1) + 4 * (2 * (sub & 1) + h)) * 4;
  }
  const int krd0 = L_K + (16 * g + (qi ^ g)) * 16;
  const int krd1 = L_K + (16 * g + (qi ^ (g + 4))) * 16;
  const int vrd = L_V + lane * 16;

  f32x4 o[8];
#pragma unroll
  for (int i = 0; i < 8; ++i) o[i] = f32x4{0.f, 0.f, 0.f, 0.f};
  float m_run = -INFINITY, l_run = 0.f;

  for (int it = 0; it < NT / RING; ++it) {
#pragma unroll
    for (int u = 0; u < RING; ++u) {
      const int t = it * RING + u;
      const int buf = (u & 1) * 16384;
      if (IS_K) {
        const f32x4 slo = *reinterpret_cast<const f32x4*>(lds + L_KS + tab_off + t * 1024);
        const f32x4 shi = *reinterpret_cast<const f32x4*>(lds + L_KS + tab_off + t * 1024 + 256);
        const f32x4 zlo = *reinterpret_cast<const f32x4*>(lds + L_KZ + tab_off + t * 1024);
        const f32x4 zhi = *reinterpret_cast<const f32x4*>(lds + L_KZ + tab_off + t * 1024 + 256);
#pragma unroll
        for (int i = 0; i < 4; ++i) {
          const u32x4 a = raw[u][2 * i], b = raw[u][2 * i + 1];
          float fa[4], fb[4];
#pragma unroll
          for (int e = 0; e < 4; ++e) {
            fa[e] = fmaf((float)(int)a[e], slo[e], zlo[e]);
            fb[e] = fmaf((float)(int)b[e], shi[e], zhi[e]);
          }
          const u32x4 w = {pk2(fa[0], fa[1]), pk2(fa[2], fa[3]), pk2(fb[0], fb[1]), pk2(fb[2], fb[3])};
          *reinterpret_cast<u32x4*>(lds + ((wr_off ^ ((i & 1) * 64)) + (i >> 1) * 128 + buf)) = w;
        }
      } else {
        const f32x4 slo = *reinterpret_cast<const f32x4*>(lds + L_VS + tab_off + t * 256);
        const f32x4 shi = *reinterpret_cast<const f32x4*>(lds + L_VS + tab_off + t * 256 + 64);
        const f32x4 zlo = *reinterpret_cast<const f32x4*>(lds + L_VZ + tab_off + t * 256);
        const f32x4 zhi = *reinterpret_cast<const f32x4*>(lds + L_VZ + tab_off + t * 256 + 64);
#pragma unroll
        for (int dd = 0; dd < 4; ++dd) {
          float f[8];
#pragma unroll
          for (int j = 0; j < 4; ++j) {
            f[j] = fmaf((float)(int)raw[u][j][dd], slo[j], zlo[j]);
            f[4 + j] = fmaf((float)(int)raw[u][4 + j][dd], shi[j], zhi[j]);
          }
          const u32x4 w = {pk2(f[0], f[1]), pk2(f[2], f[3]), pk2(f[4], f[5]), pk2(f[6], f[7])};
          *reinterpret_cast<u32x4*>(lds + (wr_off + dd * 1024 + buf)) = w;
        }
      }
      __builtin_amdgcn_sched_barrier(0);
      issue(raw[u], t + RING);
      __builtin_amdgcn_sched_barrier(0);
      __syncthreads();
      f32x4 s[4];
#pragma unroll
      for (int kb = 0; kb < 4; ++kb) {
        s[kb] = f32x4{0.f, 0.f, 0.f, 0.f};
#pragma unroll
        for (int ks = 0; ks < 4; ++ks) {
          const bf16x8 kf = *reinterpret_cast<const bf16x8*>(lds + (((ks & 1) ? krd1 : krd0) + (kb * 4 + ks) * 1024 + buf));
          s[kb] = __builtin_amdgcn_mfma_f32_16x16x32_bf16(kf, qf[ks], s[kb], 0, 0, 0);
        }
      }
      float tm = fmaxf(fmaxf(s[0][0], s[0][1]), fmaxf(s[0][2], s[0][3]));
#pragma unroll
      for (int kb = 1; kb < 4; ++kb) tm = fmaxf(tm, fmaxf(fmaxf(s[kb][0], s[kb][1]), fmaxf(s[kb][2], s[kb][3])));
      tm = xrow_max(tm) * SCALE_L2E;
      const float m_new = fmaxf(m_run, tm);
      const float alpha = __builtin_amdgcn_exp2f(m_run - m_new);
      m_run = m_new;
      float ps = 0.f;
#pragma unroll
      for (int kb = 0; kb < 4; ++kb)
#pragma unroll
        for (int i = 0; i < 4; ++i) {
          const float p = __builtin_amdgcn_exp2f(fmaf(s[kb][i], SCALE_L2E, -m_new));
          s[kb][i] = p;
          ps += p;
        }
      l_run = fmaf(l_run, alpha, ps);
#pragma unroll
      for (int i = 0; i < 8; ++i) o[i] *= alpha;
      bf16x8 pf[2];
#pragma unroll
      for (int k2 = 0; k2 < 2; ++k2) {
        const u32x4 w = {pk2(s[2 * k2][0], s[2 * k2][1]), pk2(s[2 * k2][2], s[2 * k2][3]), pk2(s[2 * k2 + 1][0], s[2 * k2 + 1][1]),
                         pk2(s[2 * k2 + 1][2], s[2 * k2 + 1][3])};
        pf[k2] = __builtin_bit_cast(bf16x8, w);
      }
#pragma unroll
      for (int k2 = 0; k2 < 2; ++k2)
#pragma unroll
        for (int db = 0; db < 8; ++db) {
          const bf16x8 vf = *reinterpret_cast<const bf16x8*>(lds + (vrd + (k2 * 8 + db) * 1024 + buf));
          o[db] = __builtin_amdgcn_mfma_f32_16x16x32_bf16(vf, pf[k2], o[db], 0, 0, 0);
        }
      __builtin_amdgcn_sched_barrier(0);
    }
  }
  l_run = xrow_sum(l_run);
  f32x4* po = reinterpret_cast<f32x4*>(ws + WS_PO) + ((size_t)(wg * 8 + wave) * 8) * 64 + lane;
#pragma unroll
  for (int dh = 0; dh < 2; ++dh)
#pragma unroll
    for (int i = 0; i < 4; ++i) po[(dh * 4 + i) * 64] = f32x4{o[4 * dh][i], o[4 * dh + 1][i], o[4 * dh + 2][i], o[4 * dh + 3][i]};
  if (lane < 16) *(reinterpret_cast<f32x2*>(ws + WS_ML) + (size_t)(wg * 8 + wave) * 16 + lane) = f32x2{m_run, l_run};
}

__global__ __launch_bounds__(512, 2) void kivi_attn_split(const unsigned short* __restrict__ q, const int* __restrict__ qk,
                                                          const int* __restrict__ qv, const unsigned short* __restrict__ ksc,
                                                          const unsigned short* __restrict__ kzr, const unsigned short* __restrict__ vsc,
                                                          const unsigned short* __restrict__ vzr, char* __restrict__ ws) {
  __shared__ __attribute__((aligned(16))) char lds[L_END];
  const int wg = blockIdx.x;
  const int bh = wg / NSPLIT, split = wg % NSPLIT;
  const int tid = threadIdx.x;
  const int wave = __builtin_amdgcn_readfirstlane(tid >> 6);
  const size_t slice = ((size_t)bh * SKV + (size_t)split * KEYS) * DH;
  if (wave < 4) {
    const __amdgpu_buffer_rsrc_t rs = __builtin_amdgcn_make_buffer_rsrc((void*)(qk + slice), 0, KEYS * DH * 4, 0x00020000);
    body<true>(lds, rs, q, ksc, kzr, vsc, vzr, ws, wg, bh, split, wave, tid);
  } else {
    const __amdgpu_buffer_rsrc_t rs = __builtin_amdgcn_make_buffer_rsrc((void*)(qv + slice), 0, KEYS * DH * 4, 0x00020000);
    body<false>(lds, rs, q, ksc, kzr, vsc, vzr, ws, wg, bh, split, wave, tid);
  }
}

__global__ __launch_bounds__(256) void kivi_attn_combine(const char* __restrict__ ws, unsigned short* __restrict__ out) {
  const int unit = blockIdx.x;
  const int bh = unit >> 3, head = unit & 7;
  const int lane = threadIdx.x & 63, wv = threadIdx.x >> 6;
  const int qi = lane & 15, g = lane >> 4, dh = wv >> 1, ip = wv & 1;
  f32x2 ml[NSPLIT];
  f32x4 a[NSPLIT][2];
#pragma unroll
  for (int s = 0; s < NSPLIT; ++s) {
    const size_t w = (size_t)((bh * NSPLIT + s) * 8 + head);
    ml[s] = *(reinterpret_cast<const f32x2*>(ws + WS_ML) + w * 16 + qi);
    const f32x4* po = reinterpret_cast<const f32x4*>(ws + WS_PO) + w * 8 * 64 + lane;
    a[s][0] = po[(dh * 4 + 2 * ip) * 64];
    a[s][1] = po[(dh * 4 + 2 * ip + 1) * 64];
  }
  float M = ml[0][0];
#pragma unroll
  for (int s = 1; s < NSPLIT; ++s) M = fmaxf(M, ml[s][0]);
  float L = 0.f;
  f32x4 r0 = {0.f, 0.f, 0.f, 0.f}, r1 = {0.f, 0.f, 0.f, 0.f};
#pragma unroll
  for (int s = 0; s < NSPLIT; ++s) {
    const float w = __builtin_amdgcn_exp2f(ml[s][0] - M);
    L = fmaf(w, ml[s][1], L);
    r0 += w * a[s][0];
    r1 += w * a[s][1];
  }
  const float inv = 1.f / L;
  r0 *= inv; r1 *= inv;
  const u32x4 w = {pk2(r0[0], r0[1]), pk2(r0[2], r0[3]), pk2(r1[0], r1[1]), pk2(r1[2], r1[3])};
  *reinterpret_cast<u32x4*>(out + ((size_t)unit * SQ + qi) * DH + 64 * dh + 16 * g + 8 * ip) = w;
}

}

extern "C" void kernel_launch(void* const* d_in, const int* in_sizes, int n_in, void* d_out, int out_size, void* d_ws, size_t ws_size,
                              hipStream_t stream) {
  const unsigned short* q = (const unsigned short*)d_in[0];
  const int* qk = (const int*)d_in[1];
  const int* qv = (const int*)d_in[2];
  const unsigned short* ksc = (const unsigned short*)d_in[3];
  const unsigned short* kzr = (const unsigned short*)d_in[4];
  const unsigned short* vsc = (const unsigned short*)d_in[5];
  const unsigned short* vzr = (const unsigned short*)d_in[6];
  kivi_attn_split<<<NWG, 512, 0, stream>>>(q, qk, qv, ksc, kzr, vsc, vzr, (char*)d_ws);
  kivi_attn_combine<<<NB * HQ, 256, 0, stream>>>((const char*)d_ws, (unsigned short*)d_out);
}
```

```cpp
#include <hip/hip_runtime.h>
#include <hip/hip_bf16.h>

namespace {

constexpr int NB = 16, HKV = 4, SQ = 16, SKV = 4096, DH = 128, NGK = 128;
constexpr int NSPLIT = 4;
constexpr int NTILE = SKV / 32;
constexpr int NT = NTILE / NSPLIT;
constexpr int TILE_B = 32 * DH * 4;
constexpr int NWG = NB * HKV * NSPLIT;
constexpr int NTHR = 768;
constexpr float SCALE = 0.08838834764831845f;
constexpr float L2E = 1.4426950408889634f;
constexpr float THR = 8.f;
constexpr int AUX_NT = 2, AUX_SC1 = 16;
#define TIMEK 0
#define EARLYB 0
#define PSLEEP 2

using bf16x8 = __attribute__((ext_vector_type(8))) short;
using f32x4 = __attribute__((ext_vector_type(4))) float;
using u32x4 = __attribute__((ext_vector_type(4))) unsigned;
using u32x2 = __attribute__((ext_vector_type(2))) unsigned;
using bf16x2 = __attribute__((ext_vector_type(2))) __bf16;
using f32x2 = __attribute__((ext_vector_type(2))) float;
typedef __attribute__((address_space(1))) unsigned long long gu64;
typedef __amdgpu_buffer_rsrc_t rsrc_t;

constexpr int L_K = 0;
constexpr int L_V = 16384;
constexpr int L_Q = 32768;
constexpr int L_END = L_Q + 8 * 4352;

constexpr size_t WS_B = TIMEK ? (500ull << 20) : 0;
constexpr size_t WS_EP = WS_B;
constexpr size_t WS_FL = WS_B + 16384;
constexpr size_t WS_PO = WS_B + 65536;
constexpr int PAYB = 1024;
constexpr int PO_WAVE = 4 * PAYB + 128;
constexpr int PO_PAIR = NSPLIT * 8 * PO_WAVE;
constexpr size_t WS_USED = 65536 + (size_t)NB * HKV * PO_PAIR;

__device__ __forceinline__ rsrc_t mk_rsrc(const void* p, int bytes) { return __builtin_amdgcn_make_buffer_rsrc((void*)p, 0, bytes, 0x00020000); }
__device__ __forceinline__ unsigned pk2(float lo, float hi) {
  f32x2 v = {lo, hi};
  bf16x2 r = __builtin_convertvector(v, bf16x2);
  return __builtin_bit_cast(unsigned, r);
}
__device__ __forceinline__ float bf_lo(unsigned w) { return __builtin_bit_cast(float, w << 16); }
__device__ __forceinline__ float bf_hi(unsigned w) { return __builtin_bit_cast(float, w & 0xffff0000u); }
__device__ __forceinline__ f32x4 bf4(unsigned a, unsigned b) { return f32x4{bf_lo(a), bf_hi(a), bf_lo(b), bf_hi(b)}; }
__device__ __forceinline__ float rnd_bf(float x) { return __builtin_bit_cast(float, pk2(0.f, x)); }
__device__ __forceinline__ unsigned deqk2(unsigned x0, unsigned x1, float z0, float z1, float s0, float s1) {
  const float a0 = rnd_bf(fmaf(__builtin_bit_cast(float, x0), 0x1p126f, z0)), a1 = rnd_bf(fmaf(__builtin_bit_cast(float, x1), 0x1p126f, z1));
  return pk2(a0 * s0, a1 * s1);
}
__device__ __forceinline__ unsigned deqv2(unsigned x0, unsigned x1, float zs0, float zs1, float s0, float s1) {
  return pk2(fmaf((float)(int)x0, s0, -zs0), fmaf((float)(int)x1, s1, -zs1));
}
__device__ __forceinline__ float max3(float a, float b, float c) { float r; asm("v_max3_f32 %0, %1, %2, %3" : "=v"(r) : "v"(a), "v"(b), "v"(c)); return r; }
__device__ __forceinline__ float max2(float a, float b) { float r; asm("v_max_f32 %0, %1, %2" : "=v"(r) : "v"(a), "v"(b)); return r; }
__device__ __forceinline__ float xrow_max(float x) {
  auto s = __builtin_amdgcn_permlane16_swap(__float_as_uint(x), __float_as_uint(x), false, false);
  x = max2(__uint_as_float(s[0]), __uint_as_float(s[1]));
  auto t = __builtin_amdgcn_permlane32_swap(__float_as_uint(x), __float_as_uint(x), false, false);
  return max2(__uint_as_float(t[0]), __uint_as_float(t[1]));
}
#define SB() __builtin_amdgcn_sched_barrier(0)

__device__ __forceinline__ void k_producer(char* __restrict__ lds, rsrc_t rs, rsrc_t rsc, rsrc_t rzr, int kp, int split, int lane) {
  const int c = lane & 15, rr = lane >> 4;
  const int voff = ((4 * kp + rr) * DH + 4 * c) * 4;
  u32x4 raw[2][8];
  u32x2 ts[2][2], tz[2][2];
  auto issue_tab = [&](int b, int tile) {
#pragma unroll
    for (int h = 0; h < 2; ++h) {
      ts[b][h] = __builtin_amdgcn_raw_buffer_load_b64(rsc, c * 8 + h * 128, tile * 256, AUX_NT);
      tz[b][h] = __builtin_amdgcn_raw_buffer_load_b64(rzr, c * 8 + h * 128, tile * 256, AUX_NT);
    }
  };
  auto issue = [&](int b, int tile, int u) {
    raw[b][2 * u] = __builtin_amdgcn_raw_buffer_load_b128(rs, voff, tile * TILE_B + u * 4096, AUX_NT);
    raw[b][2 * u + 1] = __builtin_amdgcn_raw_buffer_load_b128(rs, voff + 256, tile * TILE_B + u * 4096, AUX_NT);
  };
#pragma unroll
  for (int b = 0; b < 2; ++b) {
    issue_tab(b, 4 * b + split);
    SB();
#pragma unroll
    for (int u = 0; u < 4; ++u) { issue(b, 4 * b + split, u); SB(); }
  }
  __builtin_amdgcn_s_setprio(1);
  const int wr_off = L_K + ((4 * kp + (c >> 2)) * 64 + 16 * (c & 3) + (rr ^ (c & 7))) * 16;
  for (int i = 0; i < NT; i += 2) {
#pragma unroll
    for (int b = 0; b < 2; ++b) {
      const int tn = i + 2 < NT ? 4 * (i + b + 2) + split : NTILE;
      f32x4 slo = bf4(ts[b][0][0], ts[b][0][1]), shi = bf4(ts[b][1][0], ts[b][1][1]);
      f32x4 zlo = bf4(tz[b][0][0], tz[b][0][1]) * -0x1p-23f, zhi = bf4(tz[b][1][0], tz[b][1][1]) * -0x1p-23f;
      asm volatile("" : "+v"(slo), "+v"(shi), "+v"(zlo), "+v"(zhi));
      SB();
      issue_tab(b, tn);
      SB();
#pragma unroll
      for (int u = 0; u < 4; ++u) {
        const u32x4 a = raw[b][2 * u], h = raw[b][2 * u + 1];
        const u32x4 w = {deqk2(a[0], a[1], zlo[0], zlo[1], slo[0], slo[1]), deqk2(a[2], a[3], zlo[2], zlo[3], slo[2], slo[3]),
                         deqk2(h[0], h[1], zhi[0], zhi[1], shi[0], shi[1]), deqk2(h[2], h[3], zhi[2], zhi[3], shi[2], shi[3])};
        *reinterpret_cast<u32x4*>(lds + ((wr_off ^ ((u & 1) * 64)) + (u >> 1) * 128 + b * 8192)) = w;
        SB();
        issue(b, tn, u);
        SB();
      }
      __syncthreads();
      SB();
    }
  }
  __syncthreads();
}

__device__ __forceinline__ void v_producer(char* __restrict__ lds, rsrc_t rs, rsrc_t rsc, rsrc_t rzr, int vp, int split, int lane) {
  const int c = lane & 31, h = lane >> 5;
  const int voff = ((16 * vp + 8 * h) * DH + 4 * c) * 4;
  const int toff = (16 * vp + 8 * h) * 2;
  u32x4 raw[2][8];
  u32x4 ts[2], tz[2];
  auto issue = [&](int b, int tile) {
    ts[b] = __builtin_amdgcn_raw_buffer_load_b128(rsc, toff, tile * 64, AUX_NT);
    tz[b] = __builtin_amdgcn_raw_buffer_load_b128(rzr, toff, tile * 64, AUX_NT);
    SB();
#pragma unroll
    for (int j = 0; j < 8; ++j) raw[b][j] = __builtin_amdgcn_raw_buffer_load_b128(rs, voff + j * 512, tile * TILE_B, AUX_NT);
  };
  __syncthreads();
  SB();
  issue(0, split);
  SB();
  issue(1, 4 + split);
  SB();
  const int vr = 4 * ((c >> 1) & 3) + 2 * ((c >> 3) & 1) + (c & 1);
  const int wr_off = L_V + (4 * (c >> 4) * 64 + (vr ^ (2 * (vr >> 3))) + 16 * (2 * vp + h)) * 16;
  __builtin_amdgcn_s_setprio(1);
  for (int i = 0; i < NT; i += 2) {
#pragma unroll
    for (int b = 0; b < 2; ++b) {
      const int tn = i + 2 < NT ? 4 * (i + b + 2) + split : NTILE;
      const f32x4 slo = bf4(ts[b][0], ts[b][1]), shi = bf4(ts[b][2], ts[b][3]);
      f32x4 zlo = bf4(tz[b][0], tz[b][1]) * slo, zhi = bf4(tz[b][2], tz[b][3]) * shi;
      asm("" : "+v"(zlo), "+v"(zhi));
#pragma unroll
      for (int dd = 0; dd < 4; ++dd) {
        const u32x4* r = raw[b];
        const u32x4 w = {deqv2(r[0][dd], r[1][dd], zlo[0], zlo[1], slo[0], slo[1]), deqv2(r[2][dd], r[3][dd], zlo[2], zlo[3], slo[2], slo[3]),
                         deqv2(r[4][dd], r[5][dd], zhi[0], zhi[1], shi[0], shi[1]), deqv2(r[6][dd], r[7][dd], zhi[2], zhi[3], shi[2], shi[3])};
        *reinterpret_cast<u32x4*>(lds + (wr_off + dd * 1024 + b * 8192)) = w;
      }
      SB();
      issue(b, tn);
      SB();
      __syncthreads();
      SB();
    }
  }
}

__device__ __forceinline__ void consumer(char* __restrict__ lds, const unsigned short* __restrict__ q, char* __restrict__ ws,
                                         unsigned short* __restrict__ out, int bh, int split, int wave, int lane) {
  const int qi = lane & 15, g = lane >> 4;
  const unsigned long long tk0 = TIMEK ? __builtin_amdgcn_s_memrealtime() : 0;
  u32x2 epv = __builtin_amdgcn_raw_buffer_load_b64(mk_rsrc(ws + WS_EP + (size_t)bh * 128, 8), 0, 0, AUX_SC1);
  bf16x8 qf[4];
  {
    u32x4 qraw[4];
    const rsrc_t rq = mk_rsrc(q + (size_t)(bh * 8 + wave) * SQ * DH, SQ * DH * 2);
#pragma unroll
    for (int j = 0; j < 4; ++j) qraw[j] = __builtin_amdgcn_raw_buffer_load_b128(rq, lane * 16, j * 1024, AUX_NT);
    SB();
    if (EARLYB) __builtin_amdgcn_s_barrier();
    SB();
    char* qs = lds + L_Q + wave * 4352;
#pragma unroll
    for (int j = 0; j < 4; ++j) *reinterpret_cast<u32x4*>(qs + (4 * j + g) * 272 + qi * 16) = qraw[j];
    __builtin_amdgcn_fence(__ATOMIC_RELEASE, "wavefront");
    __builtin_amdgcn_fence(__ATOMIC_ACQUIRE, "wavefront");
#pragma unroll
    for (int ks = 0; ks < 4; ++ks) {
      const u32x2 a = *reinterpret_cast<const u32x2*>(qs + qi * 272 + (4 * ks + g) * 8);
      const u32x2 b = *reinterpret_cast<const u32x2*>(qs + qi * 272 + 128 + (4 * ks + g) * 8);
      const u32x4 w = {a[0], a[1], b[0], b[1]};
      qf[ks] = __builtin_bit_cast(bf16x8, w);
    }
  }
  asm volatile("" : "+v"(epv));
  const unsigned long long epoch = ((unsigned long long)__builtin_amdgcn_readfirstlane(epv[1]) << 32) | (unsigned)__builtin_amdgcn_readfirstlane(epv[0]);
  const unsigned long long tag = epoch * 0x9E3779B97F4A7C15ull + 0x632BE59BD9B4E019ull;
  const int krd0 = L_K + (16 * g + (qi ^ g)) * 16;
  const int krd1 = L_K + (16 * g + (qi ^ (g + 4))) * 16;
  const int vrd = L_V + (lane ^ (2 * ((lane >> 3) & 1))) * 16;

  f32x4 o[8];
#pragma unroll
  for (int i = 0; i < 8; ++i) o[i] = f32x4{0.f, 0.f, 0.f, 0.f};
  f32x4 ls = {0.f, 0.f, 0.f, 0.f};
  float m_run = -INFINITY;
  bf16x8 pf = {};
  const bf16x8 ones = {0x3f80, 0x3f80, 0x3f80, 0x3f80, 0x3f80, 0x3f80, 0x3f80, 0x3f80};

  auto step = [&](int buf, bool first) {
    bf16x8 kf[8], vf[8];
#pragma unroll
    for (int ks = 0; ks < 4; ++ks)
#pragma unroll
      for (int kb = 0; kb < 2; ++kb) kf[kb * 4 + ks] = *reinterpret_cast<const bf16x8*>(lds + (((ks & 1) ? krd1 : krd0) + (kb * 4 + ks) * 1024 + buf));
    if (!first) {
#pragma unroll
      for (int f = 0; f < 8; ++f) vf[f] = *reinterpret_cast<const bf16x8*>(lds + (vrd + f * 1024 + (buf ^ 8192)));
    }
    SB();
    f32x4 s[2];
#pragma unroll
    for (int ks = 0; ks < 4; ++ks)
#pragma unroll
      for (int kb = 0; kb < 2; ++kb)
        s[kb] = __builtin_amdgcn_mfma_f32_16x16x32_bf16(kf[kb * 4 + ks], qf[ks], ks ? s[kb] : f32x4{0.f, 0.f, 0.f, 0.f}, 0, 0, 0);
#pragma unroll
    for (int kb = 0; kb < 2; ++kb)
#pragma unroll
      for (int e = 0; e < 4; ++e) s[kb][e] = rnd_bf(rnd_bf(s[kb][e]) * (SCALE * 0x1p23f));
    const float tm = max3(max3(s[0][0], s[0][1], s[0][2]), max3(s[0][3], s[1][0], s[1][1]), max2(s[1][2], s[1][3])) * L2E;
    if (!first) {
#pragma unroll
      for (int db = 0; db < 8; ++db) o[db] = __builtin_amdgcn_mfma_f32_16x16x32_bf16(vf[db], pf, o[db], 0, 0, 0);
      ls = __builtin_amdgcn_mfma_f32_16x16x32_bf16(ones, pf, ls, 0, 0, 0);
    }
    if (__any(!(tm - m_run <= THR))) {
      const float m_new = max2(m_run, xrow_max(tm));
      const float alpha = __builtin_amdgcn_exp2f(m_run - m_new);
      m_run = m_new;
      ls[0] *= alpha;
#pragma unroll
      for (int e = 0; e < 8; ++e) o[e] *= alpha;
    }
#pragma unroll
    for (int kb = 0; kb < 2; ++kb)
#pragma unroll
      for (int e = 0; e < 4; ++e) s[kb][e] = __builtin_amdgcn_exp2f(fmaf(s[kb][e], L2E, -m_run));
    const u32x4 w = {pk2(s[0][0], s[0][1]), pk2(s[0][2], s[0][3]), pk2(s[1][0], s[1][1]), pk2(s[1][2], s[1][3])};
    pf = __builtin_bit_cast(bf16x8, w);
    SB();
  };
  if (!EARLYB) __syncthreads();
  step(0, true);
  __syncthreads();
  step(8192, false);
  __syncthreads();
  for (int i = 2; i < NT; i += 2) {
    step(0, false);
    __syncthreads();
    step(8192, false);
    __syncthreads();
  }
#pragma unroll
  for (int db = 0; db < 8; ++db)
    o[db] = __builtin_amdgcn_mfma_f32_16x16x32_bf16(*reinterpret_cast<const bf16x8*>(lds + (vrd + 8192 + db * 1024)), pf, o[db], 0, 0, 0);
  ls = __builtin_amdgcn_mfma_f32_16x16x32_bf16(ones, pf, ls, 0, 0, 0);

  const int ln = __builtin_amdgcn_mbcnt_hi(~0u, __builtin_amdgcn_mbcnt_lo(~0u, 0u));
  const rsrc_t rp = mk_rsrc(ws + WS_PO + (size_t)bh * PO_PAIR, PO_PAIR);
  gu64* fp = (gu64*)(ws + WS_FL + (size_t)(bh * NSPLIT + (ln & 3)) * 128);
  float own[8];
  {
    const int so = (split * 8 + wave) * PO_WAVE;
    __builtin_amdgcn_raw_buffer_store_b64(u32x2{__float_as_uint(m_run), __float_as_uint(ls[0])}, rp, ln < 16 ? ln * 8 + 4 * PAYB : PO_PAIR, so, AUX_SC1);
#pragma unroll
    for (int ds = 0; ds < 4; ++ds) {
      const int dh = ds >> 1, i0 = 2 * (ds & 1);
      float v[8];
#pragma unroll
      for (int e = 0; e < 4; ++e) { v[e] = o[4 * dh + e][i0]; v[4 + e] = o[4 * dh + e][i0 + 1]; }
      if (ds == split) {
#pragma unroll
        for (int e = 0; e < 8; ++e) own[e] = v[e];
      } else {
        const u32x4 w = {pk2(v[0], v[1]), pk2(v[2], v[3]), pk2(v[4], v[5]), pk2(v[6], v[7])};
        __builtin_amdgcn_raw_buffer_store_b128(w, rp, ln * 16 + ds * PAYB, so, AUX_SC1);
      }
    }
  }
  unsigned long long fl = tag;
  if (wave == 0 && ln < 4) fl = __hip_atomic_load(fp, __ATOMIC_RELAXED, __HIP_MEMORY_SCOPE_AGENT);
  asm volatile("s_waitcnt vmcnt(0)" : "+v"(fl) :: "memory");
  __syncthreads();
  if (wave == 0) {
    if (ln == split) __hip_atomic_store(fp, tag, __ATOMIC_RELAXED, __HIP_MEMORY_SCOPE_AGENT);
    for (unsigned spins = 0; spins < (1u << 15); ++spins) {
      if (__all(fl == tag || ln == split)) break;
      __builtin_amdgcn_s_sleep(PSLEEP);
      if (ln < 4) fl = __hip_atomic_load(fp, __ATOMIC_RELAXED, __HIP_MEMORY_SCOPE_AGENT);
    }
  }
  __syncthreads();
  u32x4 pa[3];
  u32x2 pm[3];
#pragma unroll
  for (int k = 0; k < 3; ++k) {
    const int so = (((split + 1 + k) & 3) * 8 + wave) * PO_WAVE;
    pm[k] = __builtin_amdgcn_raw_buffer_load_b64(rp, (ln & 15) * 8 + 4 * PAYB, so, AUX_SC1);
    pa[k] = __builtin_amdgcn_raw_buffer_load_b128(rp, ln * 16, so + split * PAYB, AUX_SC1);
  }
  float M = m_run;
#pragma unroll
  for (int k = 0; k < 3; ++k) M = fmaxf(M, __uint_as_float(pm[k][0]));
  const float w0 = __builtin_amdgcn_exp2f(m_run - M);
  float L = w0 * ls[0], wk[3];
#pragma unroll
  for (int k = 0; k < 3; ++k) { wk[k] = __builtin_amdgcn_exp2f(__uint_as_float(pm[k][0]) - M); L = fmaf(wk[k], __uint_as_float(pm[k][1]), L); }
  const float inv = 1.f / L;
  float r[8];
#pragma unroll
  for (int e = 0; e < 8; ++e) {
    float a = w0 * own[e];
#pragma unroll
    for (int k = 0; k < 3; ++k) a = fmaf(wk[k], (e & 1) ? bf_hi(pa[k][e >> 1]) : bf_lo(pa[k][e >> 1]), a);
    r[e] = a * inv;
  }
  __builtin_amdgcn_raw_buffer_store_b128(u32x4{pk2(r[0], r[1]), pk2(r[2], r[3]), pk2(r[4], r[5]), pk2(r[6], r[7])},
                                         mk_rsrc(out + (size_t)(bh * 8 + wave) * SQ * DH, SQ * DH * 2), (ln & 15) * 256 + (ln >> 4) * 16, split * 64, 0);
  if (split == 0 && wave == 0 && ln == 0) __hip_atomic_store((gu64*)(ws + WS_EP + (size_t)bh * 128), tag, __ATOMIC_RELAXED, __HIP_MEMORY_SCOPE_AGENT);
  if (TIMEK) { asm volatile("s_waitcnt vmcnt(0)" ::: "memory"); const unsigned long long tk1 = __builtin_amdgcn_s_memrealtime(); if (ln == 0) { unsigned long long* p = reinterpret_cast<unsigned long long*>(ws + (96ull << 20)) + (blockIdx.x * 8 + wave) * 2; p[0] = tk0; p[1] = tk1; } }
}

__global__ __launch_bounds__(NTHR, 3) void kivi_attn(const unsigned short* __restrict__ q, const int* __restrict__ qk,
                                                     const int* __restrict__ qv, const unsigned short* __restrict__ ksc,
                                                     const unsigned short* __restrict__ kzr, const unsigned short* __restrict__ vsc,
                                                     const unsigned short* __restrict__ vzr, char* __restrict__ ws, unsigned short* __restrict__ out) {
  __shared__ __attribute__((aligned(16))) char lds[L_END];
  const int wg = blockIdx.x;
  const int bh = 8 * (wg >> 5) + (wg & 7), split = (wg >> 3) & 3;
  const int lane = threadIdx.x & 63;
  const int wave = __builtin_amdgcn_readfirstlane(threadIdx.x >> 6);
  const size_t slice = (size_t)bh * SKV * DH;
  if (wave < 8) {
    consumer(lds, q, ws, out, bh, split, wave, lane);
  } else if (wave < 10) {
    k_producer(lds, mk_rsrc(qk + slice, SKV * DH * 4), mk_rsrc(ksc + (size_t)bh * NGK * DH, NGK * DH * 2),
               mk_rsrc(kzr + (size_t)bh * NGK * DH, NGK * DH * 2), wave - 8, split, lane);
  } else {
    v_producer(lds, mk_rsrc(qv + slice, SKV * DH * 4), mk_rsrc(vsc + (size_t)bh * SKV, SKV * 2), mk_rsrc(vzr + (size_t)bh * SKV, SKV * 2),
               wave - 10, split, lane);
  }
}

}

extern "C" void kernel_launch(void* const* d_in, const int* in_sizes, int n_in, void* d_out, int out_size, void* d_ws, size_t ws_size,
                              hipStream_t stream) {
  const unsigned short* q = (const unsigned short*)d_in[0];
  const int* qk = (const int*)d_in[1];
  const int* qv = (const int*)d_in[2];
  const unsigned short* ksc = (const unsigned short*)d_in[3];
  const unsigned short* kzr = (const unsigned short*)d_in[4];
  const unsigned short* vsc = (const unsigned short*)d_in[5];
  const unsigned short* vzr = (const unsigned short*)d_in[6];
  char* ws = (char*)d_ws + (TIMEK ? 0 : ((ws_size - WS_USED) & ~(size_t)4095));
  kivi_attn<<<NWG, NTHR, 0, stream>>>(q, qk, qv, ksc, kzr, vsc, vzr, ws, (unsigned short*)d_out);
}
```
